```python
import torch

def _softmax(x, axis=-1):
    return torch.softmax(x, dim=axis)
def _gelu(x):
    return torch.nn.functional.gelu(x, approximate="tanh")
def _silu(x):
    return torch.nn.functional.silu(x)
def _sigmoid(x):
    return torch.sigmoid(x)
def _layernorm(x, axis=-1, eps=1e-5):
    m = x.mean(dim=axis, keepdim=True)
    v = x.var(dim=axis, keepdim=True, unbiased=False)
    return (x - m) / torch.sqrt(v + eps)
def _scatter_add(tgt, idx, src, axis=0):
    return tgt.clone().index_add_(axis, idx, src)
def _segment_sum(src, seg, n):
    out = torch.zeros((n,) + src.shape[1:], dtype=src.dtype)
    return out.index_add_(0, seg, src)
def _pad(x, pw):
    flat = tuple(v for pair in reversed(pw) for v in pair)
    return torch.nn.functional.pad(x, flat)

kernel_name = 'standalone__mha_softmax__sampling'

def setup_inputs():

    _w = (lambda a: (lambda t: t.to(torch.bfloat16) if t.is_floating_point() else t)(torch.as_tensor(a)))
    B, H, Sq, Sk, D = 32, 32, 1, 2048, 128
    return {
        "q": _w((lambda *s: torch.randn(*s, dtype=torch.bfloat16) * 0.8)(B, H, Sq, D)),
        "k": _w((lambda *s: torch.randn(*s, dtype=torch.bfloat16) * 0.8)(B, H, Sk, D)),
        "v": _w((lambda *s: torch.randn(*s, dtype=torch.bfloat16) * 0.05)(B, H, Sk, D)),
    }

def reference(q, k, v):
    scale = q.shape[-1] ** -0.5
    scores = torch.einsum("bhqd,bhkd->bhqk", q, k) * scale
    p = _softmax(scores, axis=-1)
    return torch.einsum("bhqk,bhkd->bhqd", p, v)
```

```cpp
#include <hip/hip_runtime.h>
#include <hip/hip_bf16.h>
#include <stdint.h>

typedef unsigned u32x4 __attribute__((ext_vector_type(4)));
typedef float f32x2 __attribute__((ext_vector_type(2)));
typedef float f32x4 __attribute__((ext_vector_type(4)));
typedef __bf16 bf16x2 __attribute__((ext_vector_type(2)));

constexpr int D = 128, SK = 2048;
constexpr int WAVES = 4;
constexpr int NT = SK / 64 / WAVES;

__device__ __forceinline__ float dot2(unsigned k, unsigned q, float acc) {
  return __builtin_amdgcn_fdot2_f32_bf16(__builtin_bit_cast(bf16x2, k), __builtin_bit_cast(bf16x2, q), acc, false);
}
template <int CTRL> __device__ __forceinline__ float dpp(float x) {
  return __builtin_bit_cast(float, __builtin_amdgcn_mov_dpp(__builtin_bit_cast(int, x), CTRL, 0xf, 0xf, true));
}
constexpr int XOR1 = 0xB1, XOR2 = 0x4E, XOR7 = 0x141, XOR8 = 0x128, ROR4 = 0x124;
__device__ __forceinline__ float xrow16_max(float x) {
  auto s = __builtin_amdgcn_permlane16_swap(__float_as_uint(x), __float_as_uint(x), false, false);
  x = fmaxf(__uint_as_float(s[0]), __uint_as_float(s[1]));
  auto t = __builtin_amdgcn_permlane32_swap(__float_as_uint(x), __float_as_uint(x), false, false);
  return fmaxf(__uint_as_float(t[0]), __uint_as_float(t[1]));
}
__device__ __forceinline__ float xrow16_sum(float x) {
  auto s = __builtin_amdgcn_permlane16_swap(__float_as_uint(x), __float_as_uint(x), false, false);
  x = __uint_as_float(s[0]) + __uint_as_float(s[1]);
  auto t = __builtin_amdgcn_permlane32_swap(__float_as_uint(x), __float_as_uint(x), false, false);
  return __uint_as_float(t[0]) + __uint_as_float(t[1]);
}
__device__ __forceinline__ float wave_max(float x) {
  x = fmaxf(x, dpp<XOR1>(x)); x = fmaxf(x, dpp<XOR2>(x)); x = fmaxf(x, dpp<ROR4>(x)); x = fmaxf(x, dpp<XOR8>(x));
  return xrow16_max(x);
}
__device__ __forceinline__ float wave_sum(float x) {
  x += dpp<XOR1>(x); x += dpp<XOR2>(x); x += dpp<XOR7>(x); x += dpp<XOR8>(x);
  return xrow16_sum(x);
}
__device__ __forceinline__ u32x4 ldnt(const u32x4* p) { return __builtin_nontemporal_load(p); }

template <int J> __device__ __forceinline__ float bcast16(float x) {
  return __builtin_bit_cast(float, __builtin_amdgcn_mov_dpp(__builtin_bit_cast(int, x), 0x150 + J, 0xf, 0xf, true));
}

template <int J, bool MORE> struct PV {
  static __device__ __forceinline__ void run(u32x4 (&vb)[16], const u32x4* vnext, float p, f32x2 (&acc)[4]) {
    const float pj = bcast16<J>(p);
    const u32x4 v = vb[J];
    const f32x2 pp = {pj, pj};
#pragma unroll
    for (int k = 0; k < 4; ++k) {
      f32x2 vf = {__uint_as_float(v[k] << 16), __uint_as_float(v[k] & 0xffff0000u)};
      acc[k] = __builtin_elementwise_fma(pp, vf, acc[k]);
    }
    if constexpr (MORE) { vb[J] = ldnt(vnext + J * (WAVES * 64)); __builtin_amdgcn_sched_barrier(0); }
    if constexpr (J + 1 < 16) PV<J + 1, MORE>::run(vb, vnext, p, acc);
  }
};

template <bool MORE>
__device__ __forceinline__ void tile_step(u32x4 (&kb)[16], u32x4 (&vb)[16], const u32x4* kp, const u32x4* vp, const u32x4 qv, int c,
                                          float& m, float& l, f32x2 (&acc)[4]) {
  constexpr float C2 = 0.088388347648318440f * 1.4426950408889634f;
  float x[16];
#pragma unroll
  for (int j = 0; j < 16; ++j) {
    const u32x4 kk = kb[j];
    float d = dot2(kk[0], qv[0], 0.f);
    d = dot2(kk[1], qv[1], d); d = dot2(kk[2], qv[2], d); d = dot2(kk[3], qv[3], d);
    x[j] = d;
    if constexpr (MORE) { kb[j] = ldnt(kp + j * (WAVES * 64)); __builtin_amdgcn_sched_barrier(0); }
  }
#define TR_STEP(HALF, CTRL, BIT) _Pragma("unroll") for (int h = 0; h < (HALF); ++h) { \
      const float lo_ = x[h], hi_ = x[h + (HALF)]; const float t1_ = lo_ + dpp<CTRL>(lo_), t2_ = hi_ + dpp<CTRL>(hi_); \
      x[h] = (c & (BIT)) ? t2_ : t1_; }
  TR_STEP(8, XOR8, 8) TR_STEP(4, XOR7, 4) TR_STEP(2, XOR2, 2) TR_STEP(1, XOR1, 1)
#undef TR_STEP
  const float s = x[0] * C2;
  const float mn = fmaxf(m, wave_max(s));
  const float alpha = __builtin_amdgcn_exp2f(m - mn);
  const float p = __builtin_amdgcn_exp2f(s - mn);
  l = fmaf(l, alpha, p); m = mn;
  const f32x2 al2 = {alpha, alpha};
#pragma unroll
  for (int k = 0; k < 4; ++k) acc[k] *= al2;
  PV<0, MORE>::run(vb, vp, p, acc);
}

__global__ void __launch_bounds__(WAVES * 64) attn_decode_kernel(const u32x4* __restrict__ q, const u32x4* __restrict__ K,
                                                                 const u32x4* __restrict__ V, unsigned* __restrict__ out) {
  __shared__ __attribute__((aligned(16))) float lds[WAVES * (D + 4)];
  const int head = blockIdx.x;
  const int tid = threadIdx.x, lane = tid & 63, wave = __builtin_amdgcn_readfirstlane(tid >> 6), r = lane >> 4, c = lane & 15;
  const u32x4* kp = K + (size_t)head * (SK * 16) + wave * 64 + lane;
  const u32x4* vp = V + (size_t)head * (SK * 16) + wave * 64 + lane;
  const u32x4 qv = q[head * 16 + c];
  u32x4 kb[16], vb[16];
#pragma unroll
  for (int j = 0; j < 16; ++j) kb[j] = ldnt(kp + j * (WAVES * 64));
#pragma unroll
  for (int j = 0; j < 16; ++j) vb[j] = ldnt(vp + j * (WAVES * 64));
  __builtin_amdgcn_sched_barrier(0);
  float m = -INFINITY, l = 0.f;
  f32x2 acc[4];
#pragma unroll
  for (int k = 0; k < 4; ++k) acc[k] = f32x2{0.f, 0.f};
#pragma unroll 1
  for (int t = 0; t < NT - 1; ++t) {
    kp += 16 * WAVES * 64; vp += 16 * WAVES * 64;
    tile_step<true>(kb, vb, kp, vp, qv, c, m, l, acc);
  }
  tile_step<false>(kb, vb, kp, vp, qv, c, m, l, acc);
  float a[8];
#pragma unroll
  for (int k = 0; k < 4; ++k) { a[2 * k] = xrow16_sum(acc[k][0]); a[2 * k + 1] = xrow16_sum(acc[k][1]); }
  l = wave_sum(l);
  float* rec = lds + wave * (D + 4);
  if (r == 0) {
    *reinterpret_cast<f32x4*>(rec + 4 + 8 * c) = f32x4{a[0], a[1], a[2], a[3]};
    *reinterpret_cast<f32x4*>(rec + 8 + 8 * c) = f32x4{a[4], a[5], a[6], a[7]};
    if (c == 0) { rec[0] = m; rec[1] = l; }
  }
  __syncthreads();
  if (wave == 0) {
    float M = -INFINITY;
#pragma unroll
    for (int w = 0; w < WAVES; ++w) M = fmaxf(M, lds[w * (D + 4)]);
    float L = 0.f, o0 = 0.f, o1 = 0.f;
#pragma unroll
    for (int w = 0; w < WAVES; ++w) {
      const float* rw = lds + w * (D + 4);
      const float wt = __builtin_amdgcn_exp2f(rw[0] - M);
      L = fmaf(wt, rw[1], L); o0 = fmaf(wt, rw[4 + 2 * lane], o0); o1 = fmaf(wt, rw[5 + 2 * lane], o1);
    }
    const float inv = 1.f / L;
    bf16x2 o = {(__bf16)(o0 * inv), (__bf16)(o1 * inv)};
    out[head * 64 + lane] = __builtin_bit_cast(unsigned, o);
  }
}

extern "C" void kernel_launch(void* const* d_in, const int* in_sizes, int n_in, void* d_out, int out_size, void* d_ws,
                              size_t ws_size, hipStream_t stream) {
  const int heads = in_sizes[0] / D;
  attn_decode_kernel<<<heads, WAVES * 64, 0, stream>>>((const u32x4*)d_in[0], (const u32x4*)d_in[1], (const u32x4*)d_in[2],
                                                      (unsigned*)d_out);
}
```

```cpp
#include <hip/hip_runtime.h>
#include <hip/hip_bf16.h>
#include <stdint.h>

typedef unsigned u32x4 __attribute__((ext_vector_type(4)));
typedef float f32x2 __attribute__((ext_vector_type(2)));
typedef float f32x4 __attribute__((ext_vector_type(4)));
typedef __bf16 bf16x2 __attribute__((ext_vector_type(2)));

constexpr int D = 128, SK = 2048;
constexpr int HPW = 4;
constexpr int HEAD_BYTES = SK * D * 2;
constexpr int TPH = SK / 256;
constexpr int REC = D + 4;

__device__ __forceinline__ float dot2(unsigned k, unsigned q, float acc) {
  return __builtin_amdgcn_fdot2_f32_bf16(__builtin_bit_cast(bf16x2, k), __builtin_bit_cast(bf16x2, q), acc, false);
}
template <int CTRL> __device__ __forceinline__ float dpp(float x) {
  return __builtin_bit_cast(float, __builtin_amdgcn_mov_dpp(__builtin_bit_cast(int, x), CTRL, 0xf, 0xf, true));
}
constexpr int XOR1 = 0xB1, XOR2 = 0x4E, XOR7 = 0x141, XOR8 = 0x128, ROR4 = 0x124;
__device__ __forceinline__ float xrow16_max(float x) {
  auto s = __builtin_amdgcn_permlane16_swap(__float_as_uint(x), __float_as_uint(x), false, false);
  x = fmaxf(__uint_as_float(s[0]), __uint_as_float(s[1]));
  auto t = __builtin_amdgcn_permlane32_swap(__float_as_uint(x), __float_as_uint(x), false, false);
  return fmaxf(__uint_as_float(t[0]), __uint_as_float(t[1]));
}
__device__ __forceinline__ float xrow16_sum(float x) {
  auto s = __builtin_amdgcn_permlane16_swap(__float_as_uint(x), __float_as_uint(x), false, false);
  x = __uint_as_float(s[0]) + __uint_as_float(s[1]);
  auto t = __builtin_amdgcn_permlane32_swap(__float_as_uint(x), __float_as_uint(x), false, false);
  return __uint_as_float(t[0]) + __uint_as_float(t[1]);
}
__device__ __forceinline__ float wave_max(float x) {
  x = fmaxf(x, dpp<XOR1>(x)); x = fmaxf(x, dpp<XOR2>(x)); x = fmaxf(x, dpp<ROR4>(x)); x = fmaxf(x, dpp<XOR8>(x));
  return xrow16_max(x);
}
__device__ __forceinline__ float wave_sum(float x) {
  x += dpp<XOR1>(x); x += dpp<XOR2>(x); x += dpp<XOR7>(x); x += dpp<XOR8>(x);
  return xrow16_sum(x);
}
template <int J> __device__ __forceinline__ float bcast16(float x) {
  return __builtin_bit_cast(float, __builtin_amdgcn_mov_dpp(__builtin_bit_cast(int, x), 0x150 + J, 0xf, 0xf, true));
}
typedef __amdgpu_buffer_rsrc_t rsrc_t;
__device__ __forceinline__ u32x4 ldnt(rsrc_t r, unsigned voff, unsigned soff) { return __builtin_amdgcn_raw_buffer_load_b128(r, voff, soff, 2); }

template <int J> struct PV {
  static __device__ __forceinline__ void run(u32x4 (&vb)[16], rsrc_t rv, unsigned voff, unsigned so, float p, f32x2 (&acc)[4]) {
    const float pj = bcast16<J>(p);
    const u32x4 v = vb[J];
    const f32x2 pp = {pj, pj};
#pragma unroll
    for (int k = 0; k < 4; ++k) {
      f32x2 vf = {__uint_as_float(v[k] << 16), __uint_as_float(v[k] & 0xffff0000u)};
      acc[k] = __builtin_elementwise_fma(pp, vf, acc[k]);
    }
    vb[J] = ldnt(rv, voff, so + J * 4096);
    __builtin_amdgcn_sched_barrier(0);
    if constexpr (J + 1 < 16) PV<J + 1>::run(vb, rv, voff, so, p, acc);
  }
};

__global__ void __launch_bounds__(HPW * 64) attn_decode_kernel(const u32x4* __restrict__ q, const char* __restrict__ K,
                                                               const char* __restrict__ V, unsigned* __restrict__ out) {
  __shared__ __attribute__((aligned(16))) float lds[HPW * HPW * REC];
  constexpr float C2 = 0.088388347648318440f * 1.4426950408889634f;
  const int wg = blockIdx.x;
  const int tid = threadIdx.x, lane = tid & 63, wave = __builtin_amdgcn_readfirstlane(tid >> 6), r = lane >> 4, c = lane & 15;
  const rsrc_t rk = __builtin_amdgcn_make_buffer_rsrc((void*)(K + (size_t)wg * (HPW * HEAD_BYTES)), 0, HPW * HEAD_BYTES, 0x00020000);
  const rsrc_t rv = __builtin_amdgcn_make_buffer_rsrc((void*)(V + (size_t)wg * (HPW * HEAD_BYTES)), 0, HPW * HEAD_BYTES, 0x00020000);
  const unsigned voff = tid * 16;
  u32x4 kb[16], vb[16];
  u32x4 q0 = q[(wg * HPW + 0) * 16 + c], q1 = q[(wg * HPW + 1) * 16 + c], q2 = q[(wg * HPW + 2) * 16 + c], q3 = q[(wg * HPW + 3) * 16 + c];
  __builtin_amdgcn_sched_barrier(0);
#pragma unroll
  for (int j = 0; j < 16; ++j) { kb[j] = ldnt(rk, voff, j * 4096); __builtin_amdgcn_sched_barrier(0); }
#pragma unroll
  for (int j = 0; j < 16; ++j) { vb[j] = ldnt(rv, voff, j * 4096); __builtin_amdgcn_sched_barrier(0); }
  unsigned so = 0;
#pragma unroll 1
  for (int hh = 0; hh < HPW; ++hh) {
    float m = -INFINITY, l = 0.f;
    f32x2 acc[4];
#pragma unroll
    for (int k = 0; k < 4; ++k) acc[k] = f32x2{0.f, 0.f};
#pragma unroll 1
    for (int t = 0; t < TPH; ++t) {
      so += 65536;
      float x[16];
#pragma unroll
      for (int j = 0; j < 16; ++j) {
        const u32x4 kk = kb[j];
        float d = dot2(kk[0], q0[0], 0.f);
        d = dot2(kk[1], q0[1], d); d = dot2(kk[2], q0[2], d); d = dot2(kk[3], q0[3], d);
        x[j] = d;
        kb[j] = ldnt(rk, voff, so + j * 4096);
        __builtin_amdgcn_sched_barrier(0);
      }
#define TR_STEP(HALF, CTRL, BIT) _Pragma("unroll") for (int h = 0; h < (HALF); ++h) { \
      const float lo_ = x[h], hi_ = x[h + (HALF)]; const float t1_ = lo_ + dpp<CTRL>(lo_), t2_ = hi_ + dpp<CTRL>(hi_); \
      x[h] = (c & (BIT)) ? t2_ : t1_; }
      TR_STEP(8, XOR8, 8) TR_STEP(4, XOR7, 4) TR_STEP(2, XOR2, 2) TR_STEP(1, XOR1, 1)
#undef TR_STEP
      const float s = x[0] * C2;
      const float mn = fmaxf(m, wave_max(s));
      const float alpha = __builtin_amdgcn_exp2f(m - mn);
      const float p = __builtin_amdgcn_exp2f(s - mn);
      l = fmaf(l, alpha, p); m = mn;
      const f32x2 al2 = {alpha, alpha};
#pragma unroll
      for (int k = 0; k < 4; ++k) acc[k] *= al2;
      PV<0>::run(vb, rv, voff, so, p, acc);
    }
    float a[8];
#pragma unroll
    for (int k = 0; k < 4; ++k) { a[2 * k] = xrow16_sum(acc[k][0]); a[2 * k + 1] = xrow16_sum(acc[k][1]); }
    l = wave_sum(l);
    float* rec = lds + (hh * HPW + wave) * REC;
    if (r == 0) {
      *reinterpret_cast<f32x4*>(rec + 4 + 8 * c) = f32x4{a[0], a[1], a[2], a[3]};
      *reinterpret_cast<f32x4*>(rec + 8 + 8 * c) = f32x4{a[4], a[5], a[6], a[7]};
      if (c == 0) { rec[0] = m; rec[1] = l; }
    }
    q0 = q1; q1 = q2; q2 = q3;
  }
  __syncthreads();
  {
    const float* base = lds + wave * HPW * REC;
    float M = -INFINITY;
#pragma unroll
    for (int w = 0; w < HPW; ++w) M = fmaxf(M, base[w * REC]);
    float L = 0.f, o0 = 0.f, o1 = 0.f;
#pragma unroll
    for (int w = 0; w < HPW; ++w) {
      const float* rw = base + w * REC;
      const float wt = __builtin_amdgcn_exp2f(rw[0] - M);
      L = fmaf(wt, rw[1], L); o0 = fmaf(wt, rw[4 + 2 * lane], o0); o1 = fmaf(wt, rw[5 + 2 * lane], o1);
    }
    const float inv = 1.f / L;
    bf16x2 o = {(__bf16)(o0 * inv), (__bf16)(o1 * inv)};
    out[(wg * HPW + wave) * 64 + lane] = __builtin_bit_cast(unsigned, o);
  }
}

extern "C" void kernel_launch(void* const* d_in, const int* in_sizes, int n_in, void* d_out, int out_size, void* d_ws,
                              size_t ws_size, hipStream_t stream) {
  const int heads = in_sizes[0] / D;
  attn_decode_kernel<<<heads / HPW, HPW * 64, 0, stream>>>((const u32x4*)d_in[0], (const char*)d_in[1], (const char*)d_in[2],
                                                          (unsigned*)d_out);
}
```

```cpp
#include <hip/hip_runtime.h>
#include <hip/hip_bf16.h>
#include <stdint.h>

typedef unsigned u32x4 __attribute__((ext_vector_type(4)));
typedef float f32x2 __attribute__((ext_vector_type(2)));
typedef float f32x4 __attribute__((ext_vector_type(4)));
typedef __bf16 bf16x2 __attribute__((ext_vector_type(2)));
typedef __amdgpu_buffer_rsrc_t rsrc_t;

#define PATK 0x5555
#define PATV 0x6666
#define RING 8
constexpr int D = 128, SK = 2048;
constexpr int HPW = 4;
constexpr int HEAD_BYTES = SK * D * 2;
constexpr int TILE_BYTES = 65536;
constexpr int TPH = HEAD_BYTES / TILE_BYTES;
constexpr int NTILE = HPW * TPH;
constexpr int REC = D + 4;
constexpr int R = RING;

__device__ __forceinline__ float dot2(unsigned k, unsigned q, float acc) {
  return __builtin_amdgcn_fdot2_f32_bf16(__builtin_bit_cast(bf16x2, k), __builtin_bit_cast(bf16x2, q), acc, false);
}
template <int CTRL> __device__ __forceinline__ float dpp(float x) {
  return __builtin_bit_cast(float, __builtin_amdgcn_mov_dpp(__builtin_bit_cast(int, x), CTRL, 0xf, 0xf, true));
}
constexpr int XOR1 = 0xB1, XOR2 = 0x4E, XOR7 = 0x141, XOR8 = 0x128, ROR4 = 0x124;
__device__ __forceinline__ float xrow16_max(float x) {
  auto s = __builtin_amdgcn_permlane16_swap(__float_as_uint(x), __float_as_uint(x), false, false);
  x = fmaxf(__uint_as_float(s[0]), __uint_as_float(s[1]));
  auto t = __builtin_amdgcn_permlane32_swap(__float_as_uint(x), __float_as_uint(x), false, false);
  return fmaxf(__uint_as_float(t[0]), __uint_as_float(t[1]));
}
__device__ __forceinline__ float xrow16_sum(float x) {
  auto s = __builtin_amdgcn_permlane16_swap(__float_as_uint(x), __float_as_uint(x), false, false);
  x = __uint_as_float(s[0]) + __uint_as_float(s[1]);
  auto t = __builtin_amdgcn_permlane32_swap(__float_as_uint(x), __float_as_uint(x), false, false);
  return __uint_as_float(t[0]) + __uint_as_float(t[1]);
}
__device__ __forceinline__ float wave_max(float x) {
  x = fmaxf(x, dpp<XOR1>(x)); x = fmaxf(x, dpp<XOR2>(x)); x = fmaxf(x, dpp<ROR4>(x)); x = fmaxf(x, dpp<XOR8>(x));
  return xrow16_max(x);
}
__device__ __forceinline__ float wave_sum(float x) {
  x += dpp<XOR1>(x); x += dpp<XOR2>(x); x += dpp<XOR7>(x); x += dpp<XOR8>(x);
  return xrow16_sum(x);
}
template <int CTRL> __device__ __forceinline__ float comb(float a, float b, bool hi) {
  const float ta = a + dpp<CTRL>(a), tb = b + dpp<CTRL>(b);
  return hi ? tb : ta;
}
constexpr int bitrev4(int j) { return ((j & 1) << 3) | ((j & 2) << 1) | ((j & 4) >> 1) | ((j & 8) >> 3); }
template <int N> __device__ __forceinline__ void wait_vm() { asm volatile("s_waitcnt vmcnt(%0)" :: "n"(N)); }
__device__ __forceinline__ rsrc_t tile_rsrc(const char* p, int bytes) { return __builtin_amdgcn_make_buffer_rsrc((void*)p, 0, bytes, 0x00020000); }

struct St {
  u32x4 buf[R];
  u32x4 q;
  float x0, x1, x2, x3, p;
  f32x2 acc[4];
  rsrc_t cur, nxt;
  unsigned voff;
  bool b8, b4, b2, b1;
};
template <int P, int NF> __device__ __forceinline__ void refill(St& s) {
  wait_vm<NF - 1>();
  s.buf[P % R] = __builtin_amdgcn_raw_buffer_load_b128(P + R < 16 ? s.cur : s.nxt, s.voff, ((P + R) & 15) * 4096, 2);
  __builtin_amdgcn_sched_barrier(0);
}
template <int P> __device__ __forceinline__ void kstep(St& s) {
  const u32x4 d = s.buf[P % R];
  float x = dot2(d[0], s.q[0], 0.f);
  x = dot2(d[1], s.q[1], x); x = dot2(d[2], s.q[2], x); x = dot2(d[3], s.q[3], x);
  asm volatile("" : "+v"(x));
  refill<P, (PATK >> (4 * (P & 3))) & 15>(s);
  if constexpr ((P & 1) == 0) s.x0 = x;
  else {
    x = comb<XOR8>(s.x0, x, s.b8);
    if constexpr ((P & 2) == 0) s.x1 = x;
    else {
      x = comb<XOR7>(s.x1, x, s.b4);
      if constexpr ((P & 4) == 0) s.x2 = x;
      else {
        x = comb<XOR2>(s.x2, x, s.b2);
        if constexpr ((P & 8) == 0) s.x3 = x;
        else s.p = comb<XOR1>(s.x3, x, s.b1);
      }
    }
  }
  __builtin_amdgcn_sched_barrier(0);
  if constexpr (P + 1 < 16) kstep<P + 1>(s);
}
template <int P> __device__ __forceinline__ void vstep(St& s) {
  const u32x4 d = s.buf[P % R];
  f32x2 vf[4];
#pragma unroll
  for (int k = 0; k < 4; ++k) vf[k] = f32x2{__uint_as_float(d[k] << 16), __uint_as_float(d[k] & 0xffff0000u)};
  asm volatile("" : "+v"(vf[0]), "+v"(vf[1]), "+v"(vf[2]), "+v"(vf[3]));
  refill<P, (PATV >> (4 * (P & 3))) & 15>(s);
  const float pj = dpp<0x150 + bitrev4(P)>(s.p);
  const f32x2 pp = {pj, pj};
#pragma unroll
  for (int k = 0; k < 4; ++k) s.acc[k] = __builtin_elementwise_fma(pp, vf[k], s.acc[k]);
  __builtin_amdgcn_sched_barrier(0);
  if constexpr (P + 1 < 16) vstep<P + 1>(s);
}

__global__ void __launch_bounds__(HPW * 64) attn_decode_kernel(const u32x4* __restrict__ q, const char* __restrict__ K,
                                                               const char* __restrict__ V, unsigned* __restrict__ out) {
  __shared__ __attribute__((aligned(16))) float lds[HPW * HPW * REC + (NTILE + 1) * 256];
  constexpr float C2 = 0.088388347648318440f * 1.4426950408889634f;
  const int wg = blockIdx.x;
  const int tid = threadIdx.x, lane = tid & 63, wave = __builtin_amdgcn_readfirstlane(tid >> 6), r = lane >> 4, c = lane & 15;
  float* sc = lds + HPW * HPW * REC + tid;
  const char* kp = K + (size_t)wg * (HPW * HEAD_BYTES);
  const char* vp = V + (size_t)wg * (HPW * HEAD_BYTES);
  St s;
  s.voff = tid * 16;
  s.q = q[(wg * HPW + 0) * 16 + c];
  u32x4 q1 = q[(wg * HPW + 1) * 16 + c], q2 = q[(wg * HPW + 2) * 16 + c], q3 = q[(wg * HPW + 3) * 16 + c];
  __builtin_amdgcn_sched_barrier(0);
  s.nxt = tile_rsrc(kp, TILE_BYTES);
#pragma unroll
  for (int j = 0; j < R; ++j) {
    s.buf[j] = __builtin_amdgcn_raw_buffer_load_b128(s.nxt, s.voff, j * 4096, 2);
    __builtin_amdgcn_sched_barrier(0);
  }
  s.b8 = c & 8; s.b4 = c & 4; s.b2 = c & 2; s.b1 = c & 1;
#pragma unroll 1
  for (int hh = 0; hh < HPW; ++hh) {
    float mrun = -INFINITY;
#pragma unroll 1
    for (int t = 0; t < TPH; ++t) {
      const int tile = hh * TPH + t;
      kp += TILE_BYTES;
      s.cur = s.nxt; s.nxt = tile_rsrc(tile + 1 < NTILE ? kp : vp, TILE_BYTES);
      kstep<0>(s);
      const float x = s.p * C2;
      sc[tile * 256] = x;
      mrun = fmaxf(mrun, x);
    }
    mrun = wave_max(mrun);
    if (lane == 0) lds[(hh * HPW + wave) * REC] = mrun;
    s.q = q1; q1 = q2; q2 = q3;
  }
  float xn = sc[0];
#pragma unroll 1
  for (int hh = 0; hh < HPW; ++hh) {
    float* rec = lds + (hh * HPW + wave) * REC;
    const float m = rec[0];
    float l = 0.f;
#pragma unroll
    for (int k = 0; k < 4; ++k) s.acc[k] = f32x2{0.f, 0.f};
#pragma unroll 1
    for (int t = 0; t < TPH; ++t) {
      const int tile = hh * TPH + t;
      vp += TILE_BYTES;
      s.cur = s.nxt; s.nxt = tile_rsrc(vp, tile + 1 < NTILE ? TILE_BYTES : 0);
      s.p = __builtin_amdgcn_exp2f(xn - m);
      l += s.p;
      xn = sc[(tile + 1) * 256];
      vstep<0>(s);
    }
    float a[8];
#pragma unroll
    for (int k = 0; k < 4; ++k) { a[2 * k] = xrow16_sum(s.acc[k][0]); a[2 * k + 1] = xrow16_sum(s.acc[k][1]); }
    l = wave_sum(l);
    if (r == 0) {
      *reinterpret_cast<f32x4*>(rec + 4 + 8 * c) = f32x4{a[0], a[1], a[2], a[3]};
      *reinterpret_cast<f32x4*>(rec + 8 + 8 * c) = f32x4{a[4], a[5], a[6], a[7]};
      if (c == 0) rec[1] = l;
    }
  }
  __syncthreads();
  {
    const float* base = lds + wave * HPW * REC;
    float M = -INFINITY;
#pragma unroll
    for (int w = 0; w < HPW; ++w) M = fmaxf(M, base[w * REC]);
    float L = 0.f, o0 = 0.f, o1 = 0.f;
#pragma unroll
    for (int w = 0; w < HPW; ++w) {
      const float* rw = base + w * REC;
      const float wt = __builtin_amdgcn_exp2f(rw[0] - M);
      L = fmaf(wt, rw[1], L); o0 = fmaf(wt, rw[4 + 2 * lane], o0); o1 = fmaf(wt, rw[5 + 2 * lane], o1);
    }
    const float inv = 1.f / L;
    bf16x2 o = {(__bf16)(o0 * inv), (__bf16)(o1 * inv)};
    out[(wg * HPW + wave) * 64 + lane] = __builtin_bit_cast(unsigned, o);
  }
}

extern "C" void kernel_launch(void* const* d_in, const int* in_sizes, int n_in, void* d_out, int out_size, void* d_ws,
                              size_t ws_size, hipStream_t stream) {
  const int heads = in_sizes[0] / D;
  attn_decode_kernel<<<heads / HPW, HPW * 64, 0, stream>>>((const u32x4*)d_in[0], (const char*)d_in[1], (const char*)d_in[2],
                                                          (unsigned*)d_out);
}
```

```cpp
#include <hip/hip_runtime.h>
#include <hip/hip_bf16.h>
#include <stdint.h>

typedef unsigned u32x4 __attribute__((ext_vector_type(4)));
typedef unsigned u32x2 __attribute__((ext_vector_type(2)));
typedef float f32x2 __attribute__((ext_vector_type(2)));
typedef __bf16 bf16x2 __attribute__((ext_vector_type(2)));
typedef __amdgpu_buffer_rsrc_t rsrc_t;

constexpr int TAU = 13600;
constexpr int T1ST = 305;
constexpr int SH_TH = 32, SH_TH2 = 90;
constexpr int SH_MARGIN = 60, SH_MAX = 900;
constexpr int KA0 = 36, KAP = 10, KAX = 30;
constexpr int NFL = 8;
constexpr int TB2 = 70, TB3 = 120;
constexpr int NREP = 8, BOARD_STRIDE = 1088;
constexpr int D = 128, SK = 2048;
constexpr int HPW = 4;
constexpr int HEAD_BYTES = SK * D * 2;
constexpr int TILE_BYTES = 65536;
constexpr int TPH = HEAD_BYTES / TILE_BYTES;
constexpr int NTILE = HPW * TPH;
constexpr int REC = D + 4;
constexpr int AUX_NT = 2, AUX_SC1 = 16;

__device__ __forceinline__ float dot2(unsigned k, unsigned q, float acc) {
  return __builtin_amdgcn_fdot2_f32_bf16(__builtin_bit_cast(bf16x2, k), __builtin_bit_cast(bf16x2, q), acc, false);
}
template <int CTRL> __device__ __forceinline__ int dppi(int x) { return __builtin_amdgcn_mov_dpp(x, CTRL, 0xf, 0xf, true); }
template <int CTRL> __device__ __forceinline__ float dpp(float x) { return __builtin_bit_cast(float, dppi<CTRL>(__builtin_bit_cast(int, x))); }
constexpr int XOR1 = 0xB1, XOR2 = 0x4E, XOR7 = 0x141, XOR8 = 0x128, ROR4 = 0x124, BCAST = 0x150;
__device__ __forceinline__ float fold32(float a, float b) {
  auto t = __builtin_amdgcn_permlane32_swap(__float_as_uint(a), __float_as_uint(b), false, false);
  return __uint_as_float(t[0]) + __uint_as_float(t[1]);
}
__device__ __forceinline__ float fold16(float a, float b) {
  auto t = __builtin_amdgcn_permlane16_swap(__float_as_uint(a), __float_as_uint(b), false, false);
  return __uint_as_float(t[0]) + __uint_as_float(t[1]);
}
__device__ __forceinline__ float wave_max(float x) {
  x = fmaxf(x, dpp<XOR1>(x)); x = fmaxf(x, dpp<XOR2>(x)); x = fmaxf(x, dpp<ROR4>(x)); x = fmaxf(x, dpp<XOR8>(x));
  auto s = __builtin_amdgcn_permlane16_swap(__float_as_uint(x), __float_as_uint(x), false, false);
  x = fmaxf(__uint_as_float(s[0]), __uint_as_float(s[1]));
  auto t = __builtin_amdgcn_permlane32_swap(__float_as_uint(x), __float_as_uint(x), false, false);
  return fmaxf(__uint_as_float(t[0]), __uint_as_float(t[1]));
}
__device__ __forceinline__ float wave_sum(float x) {
  x += dpp<XOR1>(x); x += dpp<XOR2>(x); x += dpp<XOR7>(x); x += dpp<XOR8>(x);
  x = fold16(x, x);
  return fold32(x, x);
}
template <int CTRL> __device__ __forceinline__ float comb(float a, float b, bool hi) {
  const float ta = a + dpp<CTRL>(a), tb = b + dpp<CTRL>(b);
  return hi ? tb : ta;
}
constexpr int bitrev4(int j) { return ((j & 1) << 3) | ((j & 2) << 1) | ((j & 4) >> 1) | ((j & 8) >> 3); }
__device__ __forceinline__ rsrc_t make_rsrc(const void* p, int bytes) { return __builtin_amdgcn_make_buffer_rsrc((void*)p, 0, bytes, 0x00020000); }

struct St {
  u32x4 buf[16];
  u32x4 q;
  float x2, x3, p;
  f32x2 acc[4];
  rsrc_t nxt;
  rsrc_t board;
  u32x2 poll;
  unsigned voff, boff;
  unsigned t0;
  int sched;
  int lat, th;
  bool b8, b4, b2, b1;
};
__device__ __forceinline__ int wait_until(unsigned target) {
  int late;
  asm volatile("s_memrealtime s[60:61]\n\t"
               "s_waitcnt lgkmcnt(0)\n\t"
               "s_sub_i32 %0, s60, %1\n\t"
               "s_cmp_lt_i32 %0, 0\n\t"
               "s_cbranch_scc0 2f\n"
               "1:\n\t"
               "s_sleep 1\n\t"
               "s_memrealtime s[60:61]\n\t"
               "s_waitcnt lgkmcnt(0)\n\t"
               "s_sub_i32 s60, s60, %1\n\t"
               "s_cmp_lt_i32 s60, 0\n\t"
               "s_cbranch_scc1 1b\n"
               "2:" : "=&s"(late) : "s"(target) : "s60", "s61", "scc");
  return late;
}
template <int G> __device__ __forceinline__ void burst(St& s) {
  s.sched += TAU;
  s.lat = wait_until(s.t0 + (unsigned)(s.sched >> 8));
#pragma unroll
  for (int i = 0; i < 4; ++i) {
    asm volatile("s_waitcnt vmcnt(%0)" :: "n"(NFL - 1));
    s.buf[4 * G + i] = __builtin_amdgcn_raw_buffer_load_b128(s.nxt, s.voff, (4 * G + i) * 4096, AUX_NT);
    __builtin_amdgcn_sched_barrier(0);
  }
  if constexpr (G == 1) {
    s.poll = __builtin_amdgcn_raw_buffer_load_b64(s.board, s.boff, 0, AUX_SC1);
    __builtin_amdgcn_sched_barrier(0);
  }
}
__device__ __forceinline__ void retime(St& s, unsigned t0ref, unsigned* post, unsigned* lshift, bool sentinel, int lane) {
  const unsigned d0 = s.poll[0] - s.t0, d1 = s.poll[1] - s.t0;
  int d = max(d0 < 2048u ? (int)d0 : 0, d1 < 2048u ? (int)d1 : 0);
  d = max(d, dppi<XOR1>(d)); d = max(d, dppi<XOR2>(d));
  d = __builtin_amdgcn_readfirstlane(d);
  const int th = s.th;
  s.th = s.lat - d > SH_TH ? SH_TH : SH_TH2;
  if (sentinel && s.lat - d > th && s.t0 - t0ref < SH_MAX) {
    s.th = SH_TH2;
    d = min(s.lat + SH_MARGIN, 1024);
    if (lane < NREP) __hip_atomic_store(post, s.t0 + d, __ATOMIC_RELAXED, __HIP_MEMORY_SCOPE_AGENT);
  }
  const unsigned mine = s.t0 + d - t0ref;
  if (d != 0 && lane == 0) __hip_atomic_store(lshift, mine, __ATOMIC_RELAXED, __HIP_MEMORY_SCOPE_WORKGROUP);
  const unsigned seen = __hip_atomic_load(lshift, __ATOMIC_RELAXED, __HIP_MEMORY_SCOPE_WORKGROUP);
  s.t0 = t0ref + max(mine, (unsigned)__builtin_amdgcn_readfirstlane(seen));
}
__device__ __forceinline__ float kdot(const u32x4 d, const u32x4 q) {
  float x = dot2(d[0], q[0], 0.f);
  x = dot2(d[1], q[1], x); x = dot2(d[2], q[2], x); return dot2(d[3], q[3], x);
}
template <int G> __device__ __forceinline__ void kstep(St& s) {
  const float y0 = comb<XOR8>(kdot(s.buf[4 * G], s.q), kdot(s.buf[4 * G + 1], s.q), s.b8);
  const float y1 = comb<XOR8>(kdot(s.buf[4 * G + 2], s.q), kdot(s.buf[4 * G + 3], s.q), s.b8);
  float x = comb<XOR7>(y0, y1, s.b4);
  if constexpr ((G & 1) == 0) s.x2 = x;
  else {
    x = comb<XOR2>(s.x2, x, s.b2);
    if constexpr ((G & 2) == 0) s.x3 = x;
    else { s.p = comb<XOR1>(s.x3, x, s.b1); asm volatile("" : "+v"(s.p)); }
  }
  __builtin_amdgcn_sched_barrier(0);
  burst<G>(s);
  if constexpr (G + 1 < 4) kstep<G + 1>(s);
}
template <int P> __device__ __forceinline__ void vfma(St& s) {
  const u32x4 d = s.buf[P];
  const float pj = dpp<BCAST + bitrev4(P)>(s.p);
  const f32x2 pp = {pj, pj};
#pragma unroll
  for (int k = 0; k < 4; ++k)
    s.acc[k] = __builtin_elementwise_fma(pp, f32x2{__uint_as_float(d[k] << 16), __uint_as_float(d[k] & 0xffff0000u)}, s.acc[k]);
}
template <int G, bool MORE> __device__ __forceinline__ void vstep(St& s) {
  vfma<4 * G>(s); vfma<4 * G + 1>(s); vfma<4 * G + 2>(s); vfma<4 * G + 3>(s);
  asm volatile("" : "+v"(s.acc[0]), "+v"(s.acc[1]), "+v"(s.acc[2]), "+v"(s.acc[3]));
  __builtin_amdgcn_sched_barrier(0);
  if constexpr (MORE) burst<G>(s);
  if constexpr (G + 1 < 4) vstep<G + 1, MORE>(s);
}
__device__ __forceinline__ void flush_acc(St& s, float* rec, int r, int c) {
  const float e = fold16(fold32(s.acc[0][0], s.acc[2][0]), fold32(s.acc[1][0], s.acc[3][0]));
  const float o = fold16(fold32(s.acc[0][1], s.acc[2][1]), fold32(s.acc[1][1], s.acc[3][1]));
  *reinterpret_cast<f32x2*>(rec + 4 + 8 * c + 2 * r) = f32x2{e, o};
}

__global__ void __launch_bounds__(HPW * 64) attn_decode_kernel(const u32x4* __restrict__ q, const char* __restrict__ K,
                                                               const char* __restrict__ V, unsigned* __restrict__ out,
                                                               unsigned* ref) {
  __shared__ __attribute__((aligned(16))) float lds[HPW * HPW * REC + NTILE * 256 + 4];
  constexpr float C2 = 0.088388347648318440f * 1.4426950408889634f;
  St s;
  s.t0 = (unsigned)__builtin_amdgcn_s_memrealtime();
  unsigned warm;
  {
    unsigned long long pc; unsigned len;
    asm volatile("s_getpc_b64 %0\n.Lattn_pc:\n\ts_mov_b32 %1, .Lattn_end-.Lattn_pc" : "=s"(pc), "=s"(len));
    warm = *(const __attribute__((address_space(1))) unsigned*)(pc + min(threadIdx.x * 64u, len));
  }
  const int wg = blockIdx.x;
  const int tid = threadIdx.x, lane = tid & 63, wave = __builtin_amdgcn_readfirstlane(tid >> 6), r = lane >> 4, c = lane & 15;
  float* sc = lds + HPW * HPW * REC + tid;
  unsigned* lshift = reinterpret_cast<unsigned*>(lds + HPW * HPW * REC + NTILE * 256);
  const char* kp = K + (size_t)wg * (HPW * HEAD_BYTES);
  const char* vp = V + (size_t)wg * (HPW * HEAD_BYTES);
  const bool sentinel = wg < 8 && wave == 0;
  unsigned* post = ref + (lane & (NREP - 1)) * BOARD_STRIDE + (wg & 7);
  unsigned* myboard = ref + ((wg >> 3) & (NREP - 1)) * BOARD_STRIDE;
  if (sentinel) {
    asm volatile("" :: "s"(ref));
    const int ka = (int)((unsigned)__builtin_amdgcn_s_memrealtime() - s.t0);
    const int add = min(max(ka - KA0, 0) * KAP / 8, KAX);
    if (lane < NREP) __hip_atomic_store(post, s.t0 + add, __ATOMIC_RELAXED, __HIP_MEMORY_SCOPE_AGENT);
  }
  if (tid == 0) *lshift = 0;
  s.board = make_rsrc(myboard, wave == 0 ? 32 : 0);
  s.boff = (lane & 3) * 8;
  s.voff = tid * 16;
  s.q = q[(wg * HPW + 0) * 16 + c];
  u32x4 q1 = q[(wg * HPW + 1) * 16 + c], q2 = q[(wg * HPW + 2) * 16 + c], q3 = q[(wg * HPW + 3) * 16 + c];
  __builtin_amdgcn_sched_barrier(0);
  s.nxt = make_rsrc(kp, TILE_BYTES);
  unsigned tref, vtouch;
#pragma unroll
  for (int j = 0; j < 16; ++j) {
    if (j == 8) {
      vtouch = __builtin_amdgcn_raw_buffer_load_b32(make_rsrc(vp + (NTILE - 1) * TILE_BYTES, TILE_BYTES), 0, TILE_BYTES - 64, AUX_NT);
      __builtin_amdgcn_sched_barrier(0);
      wait_until(s.t0 + TB2);
      tref = __hip_atomic_load(myboard + (lane & 7), __ATOMIC_RELAXED, __HIP_MEMORY_SCOPE_AGENT);
      __builtin_amdgcn_sched_barrier(0);
    }
    if (j == 12) wait_until(s.t0 + TB3);
    s.buf[j] = __builtin_amdgcn_raw_buffer_load_b128(s.nxt, s.voff, j * 4096, AUX_NT);
    __builtin_amdgcn_sched_barrier(0);
  }
  {
    int d = (int)(tref - s.t0);
    for (int tries = 0; tries < 4 && __builtin_amdgcn_ballot_w64((unsigned)(d + 512) >= 1024u) != 0; ++tries)
      d = (int)(__hip_atomic_load(myboard + (lane & 7), __ATOMIC_RELAXED, __HIP_MEMORY_SCOPE_AGENT) - s.t0);
    d = (unsigned)(d + 512) < 1024u ? d : -512;
    d = max(d, dppi<XOR1>(d)); d = max(d, dppi<XOR2>(d)); d = max(d, dppi<XOR7>(d));
    d = __builtin_amdgcn_readfirstlane(d);
    s.t0 += d == -512 ? 0 : d;
  }
  asm volatile("" :: "v"(vtouch), "v"(warm));
  const unsigned t0ref = s.t0;
  s.th = SH_TH2;
  s.sched = T1ST * 256 - TAU + (((wg >> 3) + wave * 8) & 31) * TAU / 32;
  __syncthreads();
  s.b8 = c & 8; s.b4 = c & 4; s.b2 = c & 2; s.b1 = c & 1;
  float mrun = -INFINITY;
#pragma unroll 1
  for (int tile = 0; tile < NTILE; ++tile) {
    kp += TILE_BYTES;
    s.nxt = make_rsrc(tile + 1 < NTILE ? kp : vp, TILE_BYTES);
    kstep<0>(s);
    retime(s, t0ref, post, lshift, sentinel, lane);
    const float x = s.p * C2;
    sc[tile * 256] = x;
    mrun = fmaxf(mrun, x);
    if ((tile & (TPH - 1)) == TPH - 1) {
      mrun = wave_max(mrun);
      lds[((tile / TPH) * HPW + wave) * REC] = mrun;
      mrun = -INFINITY;
      s.q = q1; q1 = q2; q2 = q3;
    }
  }
  float* rec = lds + wave * REC;
  float xn = sc[0], m = rec[0], l = 0.f;
#pragma unroll
  for (int k = 0; k < 4; ++k) s.acc[k] = f32x2{0.f, 0.f};
#pragma unroll 1
  for (int tile = 0; tile < NTILE - 1; ++tile) {
    vp += TILE_BYTES;
    s.nxt = make_rsrc(vp, TILE_BYTES);
    s.p = __builtin_amdgcn_exp2f(xn - m);
    l += s.p;
    xn = sc[(tile + 1) * 256];
    vstep<0, true>(s);
    retime(s, t0ref, post, lshift, sentinel && tile + 5 < NTILE, lane);
    if ((tile & (TPH - 1)) == TPH - 1) {
      flush_acc(s, rec, r, c);
      l = wave_sum(l);
      if (lane == 0) rec[1] = l;
      rec += HPW * REC;
      m = rec[0]; l = 0.f;
#pragma unroll
      for (int k = 0; k < 4; ++k) s.acc[k] = f32x2{0.f, 0.f};
    }
  }
  s.p = __builtin_amdgcn_exp2f(xn - m);
  l = wave_sum(l + s.p);
  if (lane == 0) rec[1] = l;
  __builtin_amdgcn_sched_barrier(0);
  vstep<0, false>(s);
  flush_acc(s, rec, r, c);
  __syncthreads();
  {
    const float* base = lds + wave * HPW * REC;
    f32x2 ml[HPW], a[HPW];
#pragma unroll
    for (int w = 0; w < HPW; ++w) {
      ml[w] = *reinterpret_cast<const f32x2*>(base + w * REC);
      a[w] = *reinterpret_cast<const f32x2*>(base + w * REC + 4 + 2 * lane);
    }
    __builtin_amdgcn_sched_barrier(0);
    const float M = fmaxf(fmaxf(ml[0][0], ml[1][0]), fmaxf(ml[2][0], ml[3][0]));
    float L = 0.f; f32x2 o = {0.f, 0.f};
#pragma unroll
    for (int w = 0; w < HPW; ++w) {
      const float wt = __builtin_amdgcn_exp2f(ml[w][0] - M);
      L = fmaf(wt, ml[w][1], L);
      o = __builtin_elementwise_fma(f32x2{wt, wt}, a[w], o);
    }
    const float inv = __builtin_amdgcn_rcpf(L);
    const bf16x2 ob = {(__bf16)(o[0] * inv), (__bf16)(o[1] * inv)};
    asm volatile(".Lattn_end:");
    out[(wg * HPW + wave) * 64 + lane] = __builtin_bit_cast(unsigned, ob);
  }
}

extern "C" void kernel_launch(void* const* d_in, const int* in_sizes, int n_in, void* d_out, int out_size, void* d_ws,
                              size_t ws_size, hipStream_t stream) {
  const int heads = in_sizes[0] / D;
  attn_decode_kernel<<<heads / HPW, HPW * 64, 0, stream>>>((const u32x4*)d_in[0], (const char*)d_in[1], (const char*)d_in[2],
                                                          (unsigned*)d_out, (unsigned*)d_ws);
}
```
